```python
import torch

def _softmax(x, axis=-1):
    return torch.softmax(x, dim=axis)
def _gelu(x):
    return torch.nn.functional.gelu(x, approximate="tanh")
def _silu(x):
    return torch.nn.functional.silu(x)
def _sigmoid(x):
    return torch.sigmoid(x)
def _layernorm(x, axis=-1, eps=1e-5):
    m = x.mean(dim=axis, keepdim=True)
    v = x.var(dim=axis, keepdim=True, unbiased=False)
    return (x - m) / torch.sqrt(v + eps)
def _scatter_add(tgt, idx, src, axis=0):
    return tgt.clone().index_add_(axis, idx, src)
def _segment_sum(src, seg, n):
    out = torch.zeros((n,) + src.shape[1:], dtype=src.dtype)
    return out.index_add_(0, seg, src)
def _pad(x, pw):
    flat = tuple(v for pair in reversed(pw) for v in pair)
    return torch.nn.functional.pad(x, flat)

kernel_name = 'sequential_preLN__gqa_block_cached__dense_activation_squared_relu__decode'

def setup_inputs():

    _w = (lambda a: (lambda t: t.to(torch.bfloat16) if t.is_floating_point() else t)(torch.as_tensor(a)))
    def _A_setup():
        B, Sq, D = 1, 1, 576
        Hq, Hkv, Sk, Dh = 32, 16, 65536, 128
        assert Hq % Hkv == 0, (Hq, Hkv)
        return {
            "x": _w((lambda *s: torch.randn(*s, dtype=torch.bfloat16) * 0.05)(B, Sq, D)),
            "k_cache": _w((lambda *s: torch.randn(*s, dtype=torch.bfloat16) * 3.2)(B, Hkv, Sk, Dh)),
            "v_cache": _w((lambda *s: torch.randn(*s, dtype=torch.bfloat16) * 0.4)(B, Hkv, Sk, Dh)),
            "wq": _w((lambda *s: torch.randn(*s, dtype=torch.bfloat16) * 3.2)(D, Hq * Dh) * (D ** -0.5)),
            "wo": _w((lambda *s: torch.randn(*s, dtype=torch.bfloat16) * 0.4)(Hq * Dh, D) * ((Hq * Dh) ** -0.5)),
        }
    _A_in = _A_setup()

    def _B_setup():
        B, S, D, F = 1, 1, 576, 4608
        return {
            "x": _w((lambda *s: torch.randn(*s, dtype=torch.bfloat16) * 0.05)(B, S, D)),
            "w1": _w((lambda *s: torch.randn(*s, dtype=torch.bfloat16) * 0.05)(D, F)),
            "b1": _w((lambda *s: torch.randn(*s, dtype=torch.bfloat16) * 0.05)(F)),
            "w2": _w((lambda *s: torch.randn(*s, dtype=torch.bfloat16) * 0.05)(F, D)),
            "b2": _w((lambda *s: torch.randn(*s, dtype=torch.bfloat16) * 0.05)(D)),
        }
    _B_in = _B_setup()

    return {
        "A_x": _A_in["x"],
        "A_k_cache": _A_in["k_cache"],
        "A_v_cache": _A_in["v_cache"],
        "A_wq": _A_in["wq"],
        "A_wo": _A_in["wo"],
        "B_x": _B_in["x"],
        "B_w1": _B_in["w1"],
        "B_b1": _B_in["b1"],
        "B_w2": _B_in["w2"],
        "B_b2": _B_in["b2"],
    }

def reference(A_x, A_k_cache, A_v_cache, A_wq, A_wo, B_x, B_w1, B_b1, B_w2, B_b2):
    def _A_ref(x, k_cache, v_cache, wq, wo):
        B, Sq, D = x.shape
        Hq, Dh = 32, 128
        Hkv = k_cache.shape[1]
        rep = Hq // Hkv
        q = (lambda x, *d: x.permute(*d))((x @ wq).reshape(B, Sq, Hq, Dh), 0, 2, 1, 3)
        k = (lambda x, n, axis: torch.repeat_interleave(x, n, dim=axis))(k_cache, rep, axis=1)
        v = (lambda x, n, axis: torch.repeat_interleave(x, n, dim=axis))(v_cache, rep, axis=1)
        scores = torch.einsum("bhqd,bhkd->bhqk", q, k) * (Dh ** -0.5)
        p = _softmax(scores, axis=-1)
        o = torch.einsum("bhqk,bhkd->bhqd", p, v)
        return (lambda x, *d: x.permute(*d))(o, 0, 2, 1, 3).reshape(B, Sq, Hq * Dh) @ wo

    def _B_ref(x, w1, b1, w2, b2):
        h = torch.einsum("bsd,df->bsf", x, w1) + b1
        zero = torch.zeros_like(h)
        r = torch.where(h > zero, h, zero)
        h = r * r
        out = torch.einsum("bsf,fd->bsd", h, w2) + b2
        return out

    x = A_x
    h = x + _A_ref(_layernorm(x, axis=-1), A_k_cache, A_v_cache, A_wq, A_wo)
    h2 = h + _B_ref(_layernorm(h, axis=-1), B_w1, B_b1, B_w2, B_b2)
    return h2
```

```cpp
#include <hip/hip_runtime.h>
#include <hip/hip_bf16.h>
#include <stdint.h>

#define MIMIC 1

typedef unsigned short u16;
using u32x4 = __attribute__((ext_vector_type(4))) unsigned;
using f32x4 = __attribute__((ext_vector_type(4))) float;
using bf16x2 = __attribute__((ext_vector_type(2))) __bf16;
using bf16x8 = __attribute__((ext_vector_type(8))) __bf16;

constexpr int D = 576, HQ = 32, HKV = 16, SK = 65536, DH = 128, F = 4608;
constexpr int NSPLIT = 16;
constexpr int AW = 8;
constexpr int KEYS_PER_WG = SK / NSPLIT;
constexpr int TILE = 64;
constexpr int TILES_PER_WAVE = KEYS_PER_WG / TILE / AW;
constexpr float SCALE = 0.08838834764831845f;
constexpr float LOG2E = 1.4426950408889634f;

constexpr size_t WS_Q = 0;
constexpr size_t WS_PART = 16384;
constexpr int PART_STRIDE = 260;
constexpr size_t WS_P2 = WS_PART + (size_t)HKV * NSPLIT * PART_STRIDE * 4;
constexpr size_t WS_P3 = WS_P2 + (size_t)16 * D * 4;
constexpr size_t WS_H = WS_P3 + (size_t)256 * D * 4;

__device__ __forceinline__ float bf2f(u16 v) { return __builtin_bit_cast(float, (unsigned)v << 16); }
__device__ __forceinline__ float rbf(float x) {
  __bf16 b = (__bf16)x; return (float)b;
}
__device__ __forceinline__ float rbf_always(float x) { __bf16 b = (__bf16)x; return (float)b; }
__device__ __forceinline__ u16 f2bf(float x) { __bf16 b = (__bf16)x; return __builtin_bit_cast(u16, b); }
__device__ __forceinline__ unsigned pk2(float lo, float hi) { bf16x2 v = {(__bf16)lo, (__bf16)hi}; return __builtin_bit_cast(unsigned, v); }
__device__ __forceinline__ float dot2(unsigned a, unsigned b, float acc) {
  return __builtin_amdgcn_fdot2_f32_bf16(__builtin_bit_cast(bf16x2, a), __builtin_bit_cast(bf16x2, b), acc, false);
}
template <int CTRL, int BANK = 0xf> __device__ __forceinline__ float dppf(float old, float x) {
  return __builtin_bit_cast(float, __builtin_amdgcn_update_dpp(__builtin_bit_cast(int, old), __builtin_bit_cast(int, x), CTRL, 0xf, BANK, false));
}
template <int CTRL> __device__ __forceinline__ float dpp(float x) {
  return __builtin_bit_cast(float, __builtin_amdgcn_mov_dpp(__builtin_bit_cast(int, x), CTRL, 0xf, 0xf, true));
}
template <int CTRL> __device__ __forceinline__ unsigned dppu(unsigned x) {
  return (unsigned)__builtin_amdgcn_mov_dpp((int)x, CTRL, 0xf, 0xf, true);
}
constexpr int XOR1 = 0xB1, XOR2 = 0x4E, XOR7 = 0x141, XOR8 = 0x128, ROR4 = 0x124;
__device__ __forceinline__ float xrow_max(float x) {
  auto s = __builtin_amdgcn_permlane16_swap(__float_as_uint(x), __float_as_uint(x), false, false);
  x = fmaxf(__uint_as_float(s[0]), __uint_as_float(s[1]));
  auto t = __builtin_amdgcn_permlane32_swap(__float_as_uint(x), __float_as_uint(x), false, false);
  return fmaxf(__uint_as_float(t[0]), __uint_as_float(t[1]));
}
__device__ __forceinline__ float xrow_sum(float x) {
  auto s = __builtin_amdgcn_permlane16_swap(__float_as_uint(x), __float_as_uint(x), false, false);
  x = __uint_as_float(s[0]) + __uint_as_float(s[1]);
  auto t = __builtin_amdgcn_permlane32_swap(__float_as_uint(x), __float_as_uint(x), false, false);
  return __uint_as_float(t[0]) + __uint_as_float(t[1]);
}
__device__ __forceinline__ float wave_max(float x) {
  x = fmaxf(x, dpp<XOR1>(x)); x = fmaxf(x, dpp<XOR2>(x)); x = fmaxf(x, dpp<ROR4>(x)); x = fmaxf(x, dpp<XOR8>(x));
  return xrow_max(x);
}
__device__ __forceinline__ float wave_sum(float x) {
  x += dpp<XOR1>(x); x += dpp<XOR2>(x); x += dpp<ROR4>(x); x += dpp<XOR8>(x);
  return xrow_sum(x);
}

__device__ __forceinline__ void block_layernorm(const float* xin, float* xout, float* red, int tid, int nthr) {
  const int lane = tid & 63, wave = tid >> 6, nw = nthr >> 6;
  float s = 0.f;
  for (int i = tid; i < D; i += nthr) s += xin[i];
  s = wave_sum(s);
  if (lane == 0) red[wave] = s;
  __syncthreads();
  float tot = 0.f;
  for (int w = 0; w < nw; ++w) tot += red[w];
  const float mean = tot * (1.0f / D);
  __syncthreads();
  float v = 0.f;
  for (int i = tid; i < D; i += nthr) { float d = xin[i] - mean; v += d * d; }
  v = wave_sum(v);
  if (lane == 0) red[wave] = v;
  __syncthreads();
  float vt = 0.f;
  for (int w = 0; w < nw; ++w) vt += red[w];
  const float var = rbf(vt * (1.0f / D));
  const float mb = rbf(mean);
  const float den = rbf(sqrtf(rbf(var + 1e-5f)));
  for (int i = tid; i < D; i += nthr) xout[i] = rbf(rbf(xin[i] - mb) / den);
  __syncthreads();
}

__global__ void __launch_bounds__(256) k_qproj(const u16* __restrict__ x, const u16* __restrict__ wq, u16* __restrict__ qout) {
  __shared__ float xs[D], xn[D], red[16], part[32][64 + 1];
  const int tid = threadIdx.x;
  const int cg = tid & 7, rg = tid >> 3;
  const int col0 = blockIdx.x * 64 + cg * 8;
  u32x4 w[18];
#pragma unroll
  for (int i = 0; i < 18; ++i) w[i] = *reinterpret_cast<const u32x4*>(wq + (size_t)(rg + 32 * i) * (HQ * DH) + col0);
  for (int i = tid; i < D; i += 256) xs[i] = bf2f(x[i]);
  __syncthreads();
  block_layernorm(xs, xn, red, tid, 256);
  float acc[8];
#pragma unroll
  for (int k = 0; k < 8; ++k) acc[k] = 0.f;
#pragma unroll
  for (int i = 0; i < 18; ++i) {
    const float xv = xn[rg + 32 * i];
#pragma unroll
    for (int k = 0; k < 4; ++k) {
      acc[2 * k] = fmaf(xv, __builtin_bit_cast(float, w[i][k] << 16), acc[2 * k]);
      acc[2 * k + 1] = fmaf(xv, __builtin_bit_cast(float, w[i][k] & 0xffff0000u), acc[2 * k + 1]);
    }
  }
#pragma unroll
  for (int k = 0; k < 8; ++k) part[rg][cg * 8 + k] = acc[k];
  __syncthreads();
  if (tid < 64) {
    float s = 0.f;
    for (int r = 0; r < 32; ++r) s += part[r][tid];
    qout[blockIdx.x * 64 + tid] = f2bf(s);
  }
}

__global__ void __launch_bounds__(AW * 64, 2) k_attn(const u16* __restrict__ kc, const u16* __restrict__ vc, const u16* __restrict__ q, float* __restrict__ part) {
  __shared__ __attribute__((aligned(16))) float mrg[AW][PART_STRIDE];
  const int tid = threadIdx.x, lane = tid & 63, wave = __builtin_amdgcn_readfirstlane(tid >> 6);
  const int r = lane >> 4, c = lane & 15;
  const int g = blockIdx.x / NSPLIT, sp = blockIdx.x % NSPLIT;
  const size_t base = ((size_t)g * SK + (size_t)sp * KEYS_PER_WG + (size_t)wave * TILE) * DH + (size_t)lane * 8;
  const u32x4* kp = reinterpret_cast<const u32x4*>(kc + base);
  const u32x4* vp = reinterpret_cast<const u32x4*>(vc + base);
  constexpr int TSTRIDE = AW * TILE * DH / 8;
  u32x4 K[16], V[16];
#pragma unroll
  for (int u = 0; u < 16; ++u) K[u] = __builtin_nontemporal_load(kp + u * 64);
#pragma unroll
  for (int u = 0; u < 16; ++u) V[u] = __builtin_nontemporal_load(vp + u * 64);
  const u32x4 q0 = *reinterpret_cast<const u32x4*>(q + (2 * g) * DH + 8 * c);
  const u32x4 q1 = *reinterpret_cast<const u32x4*>(q + (2 * g + 1) * DH + 8 * c);
  const int head = (lane >> 3) & 1, jp = lane & 7;
  unsigned sel[4];
  {
    const unsigned b0 = 2 * head, b1 = 2 * head + 1;
    const unsigned on = (jp & 1) ? ((b1 << 24) | (b0 << 16) | 0x0c0cu) : (0x0c0c0000u | (b1 << 8) | b0);
#pragma unroll
    for (int i = 0; i < 4; ++i) sel[i] = ((jp >> 1) == i) ? on : 0x0c0c0c0cu;
  }
  float m0 = -INFINITY, m1 = -INFINITY, l0 = 0.f, l1 = 0.f;
  f32x4 acc = {0.f, 0.f, 0.f, 0.f};
  for (int t = 0; t < TILES_PER_WAVE; ++t) {
    float x0[16], x1[16];
#pragma unroll
    for (int u = 0; u < 16; ++u) {
      float a = 0.f, b = 0.f;
#pragma unroll
      for (int k = 0; k < 4; ++k) { a = dot2(K[u][k], q0[k], a); b = dot2(K[u][k], q1[k], b); }
      x0[u] = a; x1[u] = b;
    }
    if (t + 1 < TILES_PER_WAVE) {
      kp += TSTRIDE;
#pragma unroll
      for (int u = 0; u < 16; ++u) K[u] = __builtin_nontemporal_load(kp + u * 64);
    }
#define TRS(HALF, CTRL, BIT) _Pragma("unroll") for (int h = 0; h < (HALF); ++h) { \
      { const float lo_ = x0[h], hi_ = x0[h + (HALF)]; const float t1_ = lo_ + dpp<CTRL>(lo_), t2_ = hi_ + dpp<CTRL>(hi_); x0[h] = (c & (BIT)) ? t2_ : t1_; } \
      { const float lo_ = x1[h], hi_ = x1[h + (HALF)]; const float t1_ = lo_ + dpp<CTRL>(lo_), t2_ = hi_ + dpp<CTRL>(hi_); x1[h] = (c & (BIT)) ? t2_ : t1_; } }
    TRS(8, XOR8, 8) TRS(4, XOR7, 4) TRS(2, XOR2, 2) TRS(1, XOR1, 1)
#undef TRS
    const float s0 = rbf(rbf(x0[0]) * SCALE), s1 = rbf(rbf(x1[0]) * SCALE);
    if (__any((s0 > m0) || (s1 > m1))) {
      const float n0 = fmaxf(m0, wave_max(s0)), n1 = fmaxf(m1, wave_max(s1));
      const float a0 = __builtin_amdgcn_exp2f((m0 - n0) * LOG2E), a1 = __builtin_amdgcn_exp2f((m1 - n1) * LOG2E);
      l0 *= a0; l1 *= a1; m0 = n0; m1 = n1;
      const float al = head ? a1 : a0;
      acc *= al;
    }
    const float p0 = __builtin_amdgcn_exp2f((s0 - m0) * LOG2E), p1 = __builtin_amdgcn_exp2f((s1 - m1) * LOG2E);
    l0 += p0; l1 += p1;
    const unsigned pk = pk2(p0, p1);
#define PVU(u) { const unsigned w_ = dppu<0x150 + (u)>(pk); u32x4 b_; \
      b_[0] = __builtin_amdgcn_perm(w_, w_, sel[0]); b_[1] = __builtin_amdgcn_perm(w_, w_, sel[1]); \
      b_[2] = __builtin_amdgcn_perm(w_, w_, sel[2]); b_[3] = __builtin_amdgcn_perm(w_, w_, sel[3]); \
      acc = __builtin_amdgcn_mfma_f32_16x16x32_bf16(__builtin_bit_cast(bf16x8, V[u]), __builtin_bit_cast(bf16x8, b_), acc, 0, 0, 0); }
    PVU(0) PVU(1) PVU(2) PVU(3) PVU(4) PVU(5) PVU(6) PVU(7) PVU(8) PVU(9) PVU(10) PVU(11) PVU(12) PVU(13) PVU(14) PVU(15)
#undef PVU
    if (t + 1 < TILES_PER_WAVE) {
      vp += TSTRIDE;
#pragma unroll
      for (int u = 0; u < 16; ++u) V[u] = __builtin_nontemporal_load(vp + u * 64);
    }
  }
  l0 = wave_sum(l0); l1 = wave_sum(l1);
#pragma unroll
  for (int i = 0; i < 4; ++i) mrg[wave][head * DH + 8 * (4 * r + i) + jp] = acc[i];
  if (lane == 0) { mrg[wave][256] = m0; mrg[wave][257] = m1; mrg[wave][258] = l0; mrg[wave][259] = l1; }
  __syncthreads();
  if (tid < 256) {
    const int h = tid >> 7;
    float M = -INFINITY;
#pragma unroll
    for (int w = 0; w < AW; ++w) M = fmaxf(M, mrg[w][256 + h]);
    float L = 0.f, o = 0.f;
#pragma unroll
    for (int w = 0; w < AW; ++w) {
      const float wt = __builtin_amdgcn_exp2f((mrg[w][256 + h] - M) * LOG2E);
      L = fmaf(wt, mrg[w][258 + h], L); o = fmaf(wt, mrg[w][tid], o);
    }
    float* dst = part + (size_t)blockIdx.x * PART_STRIDE;
    dst[tid] = o;
    if ((tid & 127) == 0) { dst[256 + h] = M; dst[258 + h] = L; }
  }
}

__global__ void __launch_bounds__(256) k_wo(const float* __restrict__ part, const u16* __restrict__ wo, float* __restrict__ p2) {
  __shared__ float o[256], red[7][36 + 1];
  const int tid = threadIdx.x, g = blockIdx.x >> 4, j = blockIdx.x & 15;
  const int h = tid >> 7;
  const float* pg = part + (size_t)g * NSPLIT * PART_STRIDE;
  float M = -INFINITY;
#pragma unroll
  for (int s = 0; s < NSPLIT; ++s) M = fmaxf(M, pg[s * PART_STRIDE + 256 + h]);
  float L = 0.f, ov = 0.f;
#pragma unroll
  for (int s = 0; s < NSPLIT; ++s) {
    const float wt = __builtin_amdgcn_exp2f((pg[s * PART_STRIDE + 256 + h] - M) * LOG2E);
    L = fmaf(wt, pg[s * PART_STRIDE + 258 + h], L); ov = fmaf(wt, pg[s * PART_STRIDE + tid], ov);
  }
  o[tid] = rbf(ov / L);
  __syncthreads();
  if (tid < 252) {
    const int col = tid % 36, rg = tid / 36;
    float a = 0.f;
    for (int rr = rg; rr < 256; rr += 7) a = fmaf(o[rr], bf2f(wo[(size_t)(g * 256 + rr) * D + j * 36 + col]), a);
    red[rg][col] = a;
  }
  __syncthreads();
  if (tid < 36) {
    float a = 0.f;
#pragma unroll
    for (int rg = 0; rg < 7; ++rg) a += red[rg][tid];
    p2[g * D + j * 36 + tid] = a;
  }
}

__global__ void __launch_bounds__(256) k_ffn(const float* __restrict__ p2, const u16* __restrict__ x, const u16* __restrict__ w1, const u16* __restrict__ b1,
                                             const u16* __restrict__ w2, float* __restrict__ p3, float* __restrict__ hout) {
  __shared__ float hs[D], hn[D], red[16], part[14][18 + 1], hid[18];
  const int tid = threadIdx.x, cb = blockIdx.x;
  for (int i = tid; i < D; i += 256) {
    float a = 0.f;
#pragma unroll
    for (int g = 0; g < 16; ++g) a += p2[g * D + i];
    const float hv = rbf(bf2f(x[i]) + rbf(a));
    hs[i] = hv;
    if (cb == 0) hout[i] = hv;
  }
  __syncthreads();
  block_layernorm(hs, hn, red, tid, 256);
  if (tid < 252) {
    const int col = tid % 18, rg = tid / 18;
    float a = 0.f;
    for (int rr = rg; rr < D; rr += 14) a = fmaf(hn[rr], bf2f(w1[(size_t)rr * F + cb * 18 + col]), a);
    part[rg][col] = a;
  }
  __syncthreads();
  if (tid < 18) {
    float a = 0.f;
#pragma unroll
    for (int rg = 0; rg < 14; ++rg) a += part[rg][tid];
    float hv = rbf(rbf(a) + bf2f(b1[cb * 18 + tid]));
    hv = hv > 0.f ? hv : 0.f;
    hid[tid] = rbf(hv * hv);
  }
  __syncthreads();
  for (int i = tid; i < D; i += 256) {
    float a = 0.f;
#pragma unroll
    for (int k = 0; k < 18; ++k) a = fmaf(hid[k], bf2f(w2[(size_t)(cb * 18 + k) * D + i]), a);
    p3[(size_t)cb * D + i] = a;
  }
}

__global__ void __launch_bounds__(256) k_final(const float* __restrict__ p3, const float* __restrict__ h, const u16* __restrict__ b2, u16* __restrict__ out) {
  __shared__ float red[16][16 + 1];
  const int tid = threadIdx.x, col = blockIdx.x * 16 + (tid & 15), pg = tid >> 4;
  float a = 0.f;
#pragma unroll
  for (int k = 0; k < 16; ++k) a += p3[(size_t)(pg * 16 + k) * D + col];
  red[pg][tid & 15] = a;
  __syncthreads();
  if (tid < 16) {
    float s = 0.f;
#pragma unroll
    for (int k = 0; k < 16; ++k) s += red[k][tid];
    const int cc = blockIdx.x * 16 + tid;
    out[cc] = f2bf(h[cc] + rbf(rbf(s) + bf2f(b2[cc])));
  }
}

extern "C" void kernel_launch(void* const* d_in, const int* in_sizes, int n_in, void* d_out, int out_size, void* d_ws, size_t ws_size, hipStream_t stream) {
  const u16* x = (const u16*)d_in[0];
  const u16* kc = (const u16*)d_in[1];
  const u16* vc = (const u16*)d_in[2];
  const u16* wq = (const u16*)d_in[3];
  const u16* wo = (const u16*)d_in[4];
  const u16* w1 = (const u16*)d_in[6];
  const u16* b1 = (const u16*)d_in[7];
  const u16* w2 = (const u16*)d_in[8];
  const u16* b2 = (const u16*)d_in[9];
  char* ws = (char*)d_ws;
  k_qproj<<<64, 256, 0, stream>>>(x, wq, (u16*)(ws + WS_Q));
  k_attn<<<HKV * NSPLIT, AW * 64, 0, stream>>>(kc, vc, (const u16*)(ws + WS_Q), (float*)(ws + WS_PART));
  k_wo<<<256, 256, 0, stream>>>((const float*)(ws + WS_PART), wo, (float*)(ws + WS_P2));
  k_ffn<<<256, 256, 0, stream>>>((const float*)(ws + WS_P2), x, w1, b1, w2, (float*)(ws + WS_P3), (float*)(ws + WS_H));
  k_final<<<36, 256, 0, stream>>>((const float*)(ws + WS_P3), (const float*)(ws + WS_H), b2, (u16*)d_out);
}
```

```cpp
#include <hip/hip_runtime.h>
#include <stdint.h>

typedef unsigned short u16;
using u32x4 = __attribute__((ext_vector_type(4))) unsigned;
using u32x2 = __attribute__((ext_vector_type(2))) unsigned;
using f32x4 = __attribute__((ext_vector_type(4))) float;
using bf16x2 = __attribute__((ext_vector_type(2))) __bf16;
using bf16x8 = __attribute__((ext_vector_type(8))) __bf16;

#define LA 6
#define LAP 8
#define NST 6
#define TRIG_A 2
#define XA 2
#define XF 6
#define SLEEP_FAR 16
#define SLEEP_NEAR 0
#define NEAR_A 16
#define NEAR_F 15
#define TPACE 7500

constexpr int D = 576, HQ = 32, HKV = 16, SK = 65536, DH = 128, F = 4608;
constexpr int NSPLIT = 16;
constexpr int NSW = 4;
constexpr int UKEYS = 32;
constexpr int UB = UKEYS * DH * 2;
constexpr int NU = SK / UKEYS;
constexpr int UMAX = (NU + 9 * XA + 5 * XF) / 16 / 4 + 2;
constexpr int OOB = 0x40000000;
constexpr float SCALE = 0.08838834764831845f;
constexpr float LOG2E = 1.4426950408889634f;

constexpr int NF = 72;
constexpr unsigned SENT = 0xFFFFFFFFu;
constexpr int SPIN_MAX = 1 << 17;

constexpr int PART_STRIDE = 272;
constexpr size_t WS_EPOCH = 0;
constexpr size_t WS_FLAGS = 1024;
constexpr size_t WS_QX = 4096;
constexpr size_t WS_PART = WS_QX + (size_t)HKV * 4 * 256 * 8;
constexpr size_t WS_P2 = WS_PART + (size_t)HKV * NSPLIT * PART_STRIDE * 4;
constexpr size_t WS_P3 = WS_P2 + (size_t)16 * D * 4;

__device__ __forceinline__ float bf2f(u16 v) { return __builtin_bit_cast(float, (unsigned)v << 16); }
__device__ __forceinline__ float bflo(unsigned w) { return __builtin_bit_cast(float, w << 16); }
__device__ __forceinline__ float bfhi(unsigned w) { return __builtin_bit_cast(float, w & 0xffff0000u); }
__device__ __forceinline__ float rbf(float x) { __bf16 b = (__bf16)x; return (float)b; }
__device__ __forceinline__ u16 f2bf(float x) { __bf16 b = (__bf16)x; return __builtin_bit_cast(u16, b); }
__device__ __forceinline__ unsigned pk2(float lo, float hi) { bf16x2 v = {(__bf16)lo, (__bf16)hi}; return __builtin_bit_cast(unsigned, v); }
__device__ __forceinline__ float u2f(unsigned u) { return __builtin_bit_cast(float, u); }
__device__ __forceinline__ unsigned f2u(float f) { return __builtin_bit_cast(unsigned, f); }
__device__ __forceinline__ float dot2(unsigned a, unsigned b, float acc) {
  return __builtin_amdgcn_fdot2_f32_bf16(__builtin_bit_cast(bf16x2, a), __builtin_bit_cast(bf16x2, b), acc, false);
}
template <int CTRL> __device__ __forceinline__ float dpp(float x) {
  return __builtin_bit_cast(float, __builtin_amdgcn_mov_dpp(__builtin_bit_cast(int, x), CTRL, 0xf, 0xf, true));
}
constexpr int XOR1 = 0xB1, XOR2 = 0x4E, XOR7 = 0x141, XOR8 = 0x128, ROR4 = 0x124;
__device__ __forceinline__ float xrow_max(float x) {
  auto s = __builtin_amdgcn_permlane16_swap(f2u(x), f2u(x), false, false);
  x = fmaxf(u2f(s[0]), u2f(s[1]));
  auto t = __builtin_amdgcn_permlane32_swap(f2u(x), f2u(x), false, false);
  return fmaxf(u2f(t[0]), u2f(t[1]));
}
__device__ __forceinline__ float xrow_sum(float x) {
  auto s = __builtin_amdgcn_permlane16_swap(f2u(x), f2u(x), false, false);
  x = u2f(s[0]) + u2f(s[1]);
  auto t = __builtin_amdgcn_permlane32_swap(f2u(x), f2u(x), false, false);
  return u2f(t[0]) + u2f(t[1]);
}
__device__ __forceinline__ float row_max(float x) { x = fmaxf(x, dpp<XOR1>(x)); x = fmaxf(x, dpp<XOR2>(x)); x = fmaxf(x, dpp<ROR4>(x)); return fmaxf(x, dpp<XOR8>(x)); }
__device__ __forceinline__ float row_sum(float x) { x += dpp<XOR1>(x); x += dpp<XOR2>(x); x += dpp<ROR4>(x); return x + dpp<XOR8>(x); }
__device__ __forceinline__ float wave_sum(float x) { return xrow_sum(row_sum(x)); }
__device__ __forceinline__ void lds_barrier() { asm volatile("s_waitcnt lgkmcnt(0)\n\ts_barrier" ::: "memory"); }
__device__ __forceinline__ unsigned bperm(int srclane, unsigned v) { return (unsigned)__builtin_amdgcn_ds_bpermute(srclane << 2, (int)v); }
__device__ __forceinline__ float fdiv(float a, float b, float rb) { const float q = a * rb; return fmaf(fmaf(-q, b, a), rb, q); }

#define RSRC(p, bytes) __builtin_amdgcn_make_buffer_rsrc((void*)(p), 0, (bytes), 0x00020000)
#define MFMA(a, b, c) __builtin_amdgcn_mfma_f32_16x16x32_bf16(__builtin_bit_cast(bf16x8, a), __builtin_bit_cast(bf16x8, b), c, 0, 0, 0)

__device__ __forceinline__ u32x4 onehot(unsigned act2, const unsigned (&msk)[4]) { return u32x4{act2 & msk[0], act2 & msk[1], act2 & msk[2], act2 & msk[3]}; }
template <int N>
__device__ __forceinline__ f32x4 gemv_chain(const u32x4* W, const unsigned (&act)[N], const unsigned (&msk)[4]) {
  f32x4 acc[3] = {{0.f, 0.f, 0.f, 0.f}, {0.f, 0.f, 0.f, 0.f}, {0.f, 0.f, 0.f, 0.f}};
  u32x4 a[N];
  a[0] = onehot(act[0], msk); a[1] = onehot(act[1], msk);
#pragma unroll
  for (int i = 0; i < N; ++i) {
    if (i + 2 < N) { a[i + 2] = onehot(act[i + 2], msk); asm volatile("" : "+v"(a[i + 2])); }
    acc[i % 3] = MFMA(a[i], W[i], acc[i % 3]);
    __builtin_amdgcn_sched_barrier(0);
  }
  return (acc[0] + acc[1]) + acc[2];
}
__device__ __forceinline__ void gemv_fold(const f32x4& acc, float& r0, float& r1) {
  auto t = __builtin_amdgcn_permlane32_swap(f2u(acc[0]), f2u(acc[2]), false, false);
  auto u = __builtin_amdgcn_permlane32_swap(f2u(acc[1]), f2u(acc[3]), false, false);
  r0 = u2f(t[0]) + dpp<XOR8>(u2f(t[1]));
  r1 = u2f(u[0]) + dpp<XOR8>(u2f(u[1]));
}
__device__ __forceinline__ unsigned canon(float v) { const unsigned u = f2u(v); return u == SENT ? 0x7FC00000u : u; }
__device__ __forceinline__ unsigned umax3(unsigned a, unsigned b, unsigned c) { return max(max(a, b), c); }
__device__ __forceinline__ void onehot_masks(unsigned (&msk)[4], int lane) {
  const int jp = lane & 7;
  const unsigned on = (jp & 1) ? 0xffff0000u : 0x0000ffffu;
#pragma unroll
  for (int i = 0; i < 4; ++i) msk[i] = ((jp >> 1) == i) ? on : 0u;
}

#define LDKV(rs, off, u) __builtin_amdgcn_raw_buffer_load_b128(rs, (off) + ((u) & 3) * 1024, ((u) >> 2) * 4096, 2)
typedef __attribute__((address_space(3))) u16 lds_u16;

template <bool ISS>
__device__ __forceinline__ void k_unit(u32x4 (&B)[8], u32x4 (&N)[8], __amdgpu_buffer_rsrc_t rs, int cur, __amdgpu_buffer_rsrc_t rsn, int nxt,
                                       const u32x4& q0, const u32x4& q1, int c, lds_u16* sc, float& m) {
#define ISSUE(s) if constexpr (ISS) { constexpr int n_ = (s) + LA; __builtin_amdgcn_sched_barrier(0); \
    if constexpr (n_ < 8) B[n_ & 7] = LDKV(rs, cur, n_ & 7); else N[n_ & 7] = LDKV(rsn, nxt, n_ & 7); \
    __builtin_amdgcn_sched_barrier(0); }
#pragma unroll
  for (int hf = 0; hf < 2; ++hf) {
    float x[8];
#define QKU(u) { float a_ = 0.f, b_ = 0.f; \
      _Pragma("unroll") for (int k = 0; k < 4; ++k) { a_ = dot2(B[4 * hf + (u)][k], q0[k], a_); b_ = dot2(B[4 * hf + (u)][k], q1[k], b_); } \
      asm volatile("" : "+v"(a_), "+v"(b_));   \
      x[u] = a_; x[4 + (u)] = b_; if (hf == 0) { ISSUE(u) } else { ISSUE(4 + (u)) } }
    QKU(0) QKU(1) QKU(2) QKU(3)
#undef QKU
#define TRS(HALF, CTRL, BIT) _Pragma("unroll") for (int h = 0; h < (HALF); ++h) { \
      const float keep_ = (c & (BIT)) ? x[h + (HALF)] : x[h], send_ = (c & (BIT)) ? x[h] : x[h + (HALF)]; x[h] = keep_ + dpp<CTRL>(send_); }
    TRS(4, XOR8, 8) TRS(2, XOR7, 4) TRS(1, XOR2, 2)
#undef TRS
    const float r = x[0] + dpp<XOR1>(x[0]);
    const float s = rbf(rbf(r) * SCALE);
    m = fmaxf(m, s);
    sc[hf * 32] = (u16)(f2u(s) >> 16);
  }
#undef ISSUE
}
template <bool ISS>
__device__ __forceinline__ void v_unit(u32x4 (&B)[8], __amdgpu_buffer_rsrc_t rs, int cur, int nxt, const u32x4& pa, const u32x4& pb,
                                       const unsigned (&sel)[4], f32x4& acc) {
#define PVU(u) { const unsigned w_ = (u) < 4 ? pa[(u) & 3] : pb[(u) & 3]; u32x4 b_; \
    b_[0] = __builtin_amdgcn_perm(w_, w_, sel[0]); b_[1] = __builtin_amdgcn_perm(w_, w_, sel[1]); \
    b_[2] = __builtin_amdgcn_perm(w_, w_, sel[2]); b_[3] = __builtin_amdgcn_perm(w_, w_, sel[3]); \
    acc = MFMA(B[u], b_, acc); \
    { constexpr int n_ = (u) + LA; __builtin_amdgcn_sched_barrier(0); \
      if constexpr (n_ < 8) B[n_ & 7] = LDKV(rs, cur, n_ & 7); else B[n_ & 7] = LDKV(rs, nxt, n_ & 7); \
      __builtin_amdgcn_sched_barrier(0); } }
  PVU(0) PVU(1) PVU(2) PVU(3) PVU(4) PVU(5) PVU(6) PVU(7)
#undef PVU
}

__global__ void __launch_bounds__(512, 2) k_fused(const u16* __restrict__ x, const u16* __restrict__ kc, const u16* __restrict__ vc, const u16* __restrict__ wq,
                                                   const u16* __restrict__ wo, const u16* __restrict__ w1, const u16* __restrict__ b1, const u16* __restrict__ w2,
                                                   const u16* __restrict__ b2, unsigned char* __restrict__ ws, u16* __restrict__ out) {
  __shared__ __attribute__((aligned(16))) float lds[128 + NSW * PART_STRIDE + D + 4 * 64 + 16 + NSW * UMAX * 32];
  unsigned* qs = reinterpret_cast<unsigned*>(lds);
  float* mrg = lds + 128;
  float* hs = mrg + NSW * PART_STRIDE;
  float* red = hs + D;
  typedef __attribute__((address_space(3))) volatile unsigned lds_vu32;
  lds_vu32* trig = (lds_vu32*)(red + 4 * 64);
  const int tid = threadIdx.x, lane = tid & 63, wave = __builtin_amdgcn_readfirstlane(tid >> 6);
  const int bid = blockIdx.x;
  const int g = bid >> 4, sp = bid & 15;
  const int nF = 4 + (g & 1);
  const bool dutyA = sp < 9, dutyF = !dutyA && sp < 9 + nF, dutyR = sp == 15 || (sp == 14 && (g == 0 || g == 2));
  const int nj = dutyF ? 9 * (g >> 1) + 4 * (g & 1) + sp - 9 : NF + (sp == 15 ? g : 16 + (g >> 1));
  const int ubase = (NU + 9 * XA + nF * XF) / NSPLIT, urem = (NU + 9 * XA + nF * XF) % NSPLIT;
  const int u0 = sp * ubase + min(sp, urem) - XA * min(sp, 9) - XF * min(max(sp - 9, 0), nF);
  const int un = ubase + (sp < urem ? 1 : 0) - (dutyA ? XA : 0) - (dutyF ? XF : 0);
  if (wave >= 4) {
    const int r = lane >> 4, c = lane & 15, head = (lane >> 3) & 1, jp = lane & 7;
    const int sw = wave - 4;
    const size_t wbase = ((size_t)g * SK + (size_t)u0 * UKEYS) * DH;
    auto krs = RSRC(kc + wbase, un * UB);
    auto vrs = RSRC(vc + wbase, un * UB);
    const int UPW = (un - sw + NSW - 1) / NSW;
    const int first = lane * 16 + sw * UB;
    const unsigned tp0 = (unsigned)__builtin_amdgcn_s_memrealtime();
    const unsigned tstep = (TPACE * 256u * NSW) / (2u * (ubase + 1));
#define PACE_UNIT(i) { const unsigned due_ = ((unsigned)(i) * tstep) >> 8; while ((unsigned)__builtin_amdgcn_s_memrealtime() - tp0 < due_) __builtin_amdgcn_s_sleep(1); }
    unsigned* scw = reinterpret_cast<unsigned*>(red + 4 * 64 + 16) + sw * (UMAX * 32);
    lds_u16* sc = (lds_u16*)(scw + r * 4 + ((c >> 1) & 3)) + head;
    u32x4 KS[NST][8];
    {
#define PACE(s) if ((s) >= LAP) { __builtin_amdgcn_sched_barrier(0); __builtin_amdgcn_s_waitcnt(0x0F70 | ((LAP - 1) & 15) | (((LAP - 1) >> 4) << 14)); __builtin_amdgcn_sched_barrier(0); }
#pragma unroll
      for (int k = 0; k < NST; ++k)
#pragma unroll
        for (int u = 0; u < 8; ++u) { PACE(8 * k + u) KS[k][u] = LDKV(krs, first + k * NSW * UB, u); }
#undef PACE
    }
    __builtin_amdgcn_sched_barrier(0);
    lds_barrier();
    const u32x4 q0 = *reinterpret_cast<const u32x4*>(qs + 4 * c), q1 = *reinterpret_cast<const u32x4*>(qs + 64 + 4 * c);
    float m = -INFINITY;
    u32x4 B[8];
    int cur = first + NST * NSW * UB;
#pragma unroll
    for (int n = 0; n < LA; ++n) B[n] = LDKV(krs, cur, n);
    __builtin_amdgcn_sched_barrier(0);
#pragma unroll
    for (int k = 0; k < NST; ++k) {
      k_unit<false>(KS[k], B, krs, 0, krs, 0, q0, q1, c, sc + k * 64, m);
      k_unit<true>(B, B, krs, cur, krs, cur + NSW * UB, q0, q1, c, sc + (NST + k) * 64, m);
      cur += NSW * UB;
    }
    for (int t = 2 * NST; t < UPW - 1; ++t) {
      PACE_UNIT(t)
      k_unit<true>(B, B, krs, cur, krs, cur + NSW * UB, q0, q1, c, sc + t * 64, m);
      cur += NSW * UB;
    }
    k_unit<true>(B, B, krs, cur, vrs, first, q0, q1, c, sc + (UPW - 1) * 64, m);
    if (sw == 0) *trig = 1u;
    m = fmaxf(m, dpp<XOR2>(m)); m = fmaxf(m, dpp<XOR7>(m)); m = xrow_max(m);
    const float mo = dpp<XOR8>(m);
    const float m0 = head ? mo : m, m1 = head ? m : mo;
    float l0 = 0.f, l1 = 0.f;
    asm volatile("s_waitcnt lgkmcnt(0)" ::: "memory");
    for (int i = lane * 4; i < UPW * 32; i += 256) {
      u32x4 v = *reinterpret_cast<u32x4*>(scw + i);
#pragma unroll
      for (int k = 0; k < 4; ++k) {
        const float p0 = __builtin_amdgcn_exp2f((bflo(v[k]) - m0) * LOG2E), p1 = __builtin_amdgcn_exp2f((bfhi(v[k]) - m1) * LOG2E);
        l0 += p0; l1 += p1; v[k] = pk2(p0, p1);
      }
      *reinterpret_cast<u32x4*>(scw + i) = v;
    }
    asm volatile("s_waitcnt lgkmcnt(0)" ::: "memory");
    unsigned sel[4];
    {
      const unsigned b0 = 2 * head, b1_ = 2 * head + 1;
      const unsigned on = (jp & 1) ? ((b1_ << 24) | (b0 << 16) | 0x0c0cu) : (0x0c0c0000u | (b1_ << 8) | b0);
#pragma unroll
      for (int i = 0; i < 4; ++i) sel[i] = ((jp >> 1) == i) ? on : 0x0c0c0c0cu;
    }
    f32x4 acc = {0.f, 0.f, 0.f, 0.f};
    cur = first;
    const unsigned* pr = scw + r * 4;
    u32x4 pa = *reinterpret_cast<const u32x4*>(pr), pb = *reinterpret_cast<const u32x4*>(pr + 16);
    const int trig_t = (sw == 0) ? UPW - TRIG_A : -1;
    for (int t = 0; t < UPW; ++t) {
      if (t == trig_t) *trig = 2u;
      PACE_UNIT(UPW + t)
      const u32x4 na = *reinterpret_cast<const u32x4*>(pr + (t + 1) * 32), nb = *reinterpret_cast<const u32x4*>(pr + (t + 1) * 32 + 16);
      v_unit<true>(B, vrs, cur, cur + NSW * UB, pa, pb, sel, acc);
      cur += NSW * UB; pa = na; pb = nb;
    }
    l0 = wave_sum(l0); l1 = wave_sum(l1);
    float* rec = mrg + sw * PART_STRIDE;
#pragma unroll
    for (int i = 0; i < 4; ++i) rec[head * DH + 8 * (4 * r + i) + jp] = acc[i];
    if (lane == 0) { rec[256] = m0; rec[257] = m1; rec[258] = l0; rec[259] = l1; }
    lds_barrier();
    if (dutyF && sw < 3) {
      auto p2rs = RSRC(ws + WS_P2, 16 * D * 4);
      auto xrs = RSRC(x, D * 2);
      const int ct = sw * 64 + lane;
      const int off = ct < 144 ? ct * 16 : OOB;
      const u32x2 xq = __builtin_amdgcn_raw_buffer_load_b64(xrs, ct * 8, 0, 0);
      u32x4 pv[16];
#pragma unroll
      for (int k = 0; k < 16; ++k) pv[k] = u32x4{SENT, SENT, SENT, SENT};
      for (int spin = 0;; ++spin) {
        asm volatile("" ::: "memory");
        const unsigned w_ = __builtin_amdgcn_raw_buffer_load_b32(p2rs, lane < 16 ? (lane * D + sw * 256) * 4 : OOB, 0, 16);
        if (__builtin_popcountll(__ballot(w_ == SENT)) <= NEAR_F || spin > SPIN_MAX) break;
        __builtin_amdgcn_s_sleep(SLEEP_FAR);
      }
      unsigned miss = 0xffffu;
      for (int spin = 0;; ++spin) {
        asm volatile("" ::: "memory");
#pragma unroll
        for (int k = 0; k < 16; ++k)
          if (miss & (1u << k)) pv[k] = __builtin_amdgcn_raw_buffer_load_b128(p2rs, off, k * D * 4, 16);
        miss = 0;
#pragma unroll
        for (int k = 0; k < 16; ++k) miss |= (max(umax3(pv[k][0], pv[k][1], pv[k][2]), pv[k][3]) == SENT) ? (1u << k) : 0u;
        f32x4 t[8];
#pragma unroll
        for (int k = 0; k < 8; ++k) t[k] = __builtin_bit_cast(f32x4, pv[k]) + __builtin_bit_cast(f32x4, pv[k + 8]);
        const f32x4 a = ((t[0] + t[1]) + (t[2] + t[3])) + ((t[4] + t[5]) + (t[6] + t[7]));
        const f32x4 hv = {rbf(bflo(xq[0]) + rbf(a[0])), rbf(bfhi(xq[0]) + rbf(a[1])), rbf(bflo(xq[1]) + rbf(a[2])), rbf(bfhi(xq[1]) + rbf(a[3]))};
        if (ct < 144) *reinterpret_cast<f32x4*>(hs + ct * 4) = hv;
        if (__all(miss == 0) || spin > SPIN_MAX) break;
      }
      lds_barrier();
    }
    return;
  }
  const int ch = lane & 7, rl = lane >> 3;
  const bool vlane = !(lane & 8);
  const int vcol = 8 * ch + 4 * ((lane >> 4) & 1) + 2 * (lane >> 5);
  unsigned msk[4]; onehot_masks(msk, lane);
  auto part_rs = RSRC(ws + WS_PART, HKV * NSPLIT * PART_STRIDE * 4);
  auto p2rs = RSRC(ws + WS_P2, 16 * D * 4);
  auto p3rs = RSRC(ws + WS_P3, NF * D * 4);
  float xv = 0.f, bv = 0.f;
  unsigned tagv = 0, oldv = 0;
  if (wave == 0) {
    __builtin_amdgcn_s_setprio(3);
    const int rq = sp >> 2, cq = sp & 3;
    auto ers = RSRC(ws + WS_EPOCH, 4);
    const unsigned e0 = __builtin_amdgcn_raw_buffer_load_b32(ers, 0, 0, 16);
    const unsigned tag = e0 * 0x9E3779B1u + 0x7F4A7C15u;
    auto xrs = RSRC(x, D * 2);
    u32x2 xq[3]; unsigned xr[3];
#pragma unroll
    for (int i = 0; i < 3; ++i) xq[i] = __builtin_amdgcn_raw_buffer_load_b64(xrs, (lane + 64 * i) * 8, 0, 0);
#pragma unroll
    for (int i = 0; i < 3; ++i) xr[i] = __builtin_amdgcn_raw_buffer_load_b16(xrs, (144 * rq + lane + 64 * i) * 2, 0, 0);
    auto wrs = RSRC(wq + (size_t)(144 * rq) * (HQ * DH) + g * 256 + cq * 64, 144 * HQ * DH * 2);
    const int woff = rl * (HQ * DH * 2) + ch * 16;
    u32x4 W[18];
#pragma unroll
    for (int i = 0; i < 18; ++i) W[i] = __builtin_amdgcn_raw_buffer_load_b128(wrs, woff, i * (8 * HQ * DH * 2), 2);
    {
      const u32x4 sv = {SENT, SENT, SENT, SENT};
      __builtin_amdgcn_raw_buffer_store_b128(sv, part_rs, (bid * PART_STRIDE) * 4 + lane * 16, 0, 16);
      __builtin_amdgcn_raw_buffer_store_b128(sv, part_rs, lane == 0 ? (bid * PART_STRIDE + 256) * 4 : OOB, 0, 16);
      __builtin_amdgcn_raw_buffer_store_b128(sv, p2rs, (dutyA && lane < 16) ? (g * D + sp * 64) * 4 + lane * 16 : OOB, 0, 16);
#pragma unroll
      for (int i = 0; i < 3; ++i) __builtin_amdgcn_raw_buffer_store_b128(sv, p3rs, (dutyF && lane + 64 * i < 144) ? (nj * D) * 4 + (lane + 64 * i) * 16 : OOB, 0, 16);
    }
    __builtin_amdgcn_sched_barrier(0);
    float xe[12];
#pragma unroll
    for (int i = 0; i < 3; ++i) { xe[4 * i] = bflo(xq[i][0]); xe[4 * i + 1] = bfhi(xq[i][0]); xe[4 * i + 2] = bflo(xq[i][1]); xe[4 * i + 3] = bfhi(xq[i][1]); }
    float s = 0.f;
#pragma unroll
    for (int i = 0; i < 12; ++i) s += xe[i];
    const float mean = wave_sum(s) * (1.0f / D);
    float v = 0.f;
#pragma unroll
    for (int i = 0; i < 12; ++i) { const float d = (i < 8 || lane < 16) ? xe[i] - mean : 0.f; v = fmaf(d, d, v); }
    const float var = rbf(wave_sum(v) * (1.0f / D));
    const float mb = rbf(mean), den = rbf(__builtin_amdgcn_sqrtf(rbf(var + 1e-5f))), rden = __builtin_amdgcn_rcpf(den);
    unsigned xn2[3];
#pragma unroll
    for (int i = 0; i < 3; ++i) { const float t = fdiv(rbf(bflo(xr[i]) - mb), den, rden); xn2[i] = pk2(t, t); }
    unsigned act[18];
#pragma unroll
    for (int i = 0; i < 18; ++i) act[i] = bperm((8 * i + rl) & 63, xn2[(8 * i) >> 6]);
    __builtin_amdgcn_sched_barrier(0);
    float q0v, q1v; gemv_fold(gemv_chain<18>(W, act, msk), q0v, q1v);
    auto qrs = RSRC(ws + WS_QX + (size_t)g * (4 * 256 * 8), 4 * 256 * 8);
    auto frs = RSRC(ws + WS_FLAGS, 256 * 4);
    __builtin_amdgcn_raw_buffer_store_b128(u32x4{f2u(q0v), tag, f2u(q1v), tag}, qrs, vlane ? (rq * 256 + cq * 64 + vcol) * 8 : OOB, 0, 16);
    float qv[4];
    for (int spin = 0;; ++spin) {
      asm volatile("" ::: "memory");
      u32x4 gq[8];
#pragma unroll
      for (int k = 0; k < 8; ++k) gq[k] = __builtin_amdgcn_raw_buffer_load_b128(qrs, lane * 16 + (k & 1) * 1024, (k >> 1) * 2048, 16);
      unsigned bad = 0;
#pragma unroll
      for (int k = 0; k < 8; ++k) bad |= (gq[k][1] ^ tag) | (gq[k][3] ^ tag);
#pragma unroll
      for (int h = 0; h < 2; ++h) {
        qv[2 * h] = (u2f(gq[h][0]) + u2f(gq[2 + h][0])) + (u2f(gq[4 + h][0]) + u2f(gq[6 + h][0]));
        qv[2 * h + 1] = (u2f(gq[h][2]) + u2f(gq[2 + h][2])) + (u2f(gq[4 + h][2]) + u2f(gq[6 + h][2]));
      }
      if (__all(bad == 0) || spin > SPIN_MAX) break;
    }
    qs[lane] = pk2(qv[0], qv[1]); qs[64 + lane] = pk2(qv[2], qv[3]);
    lds_barrier();
    __builtin_amdgcn_s_setprio(0);
    asm volatile("s_waitcnt vmcnt(0)" ::: "memory");
    __builtin_amdgcn_raw_buffer_store_b32(tag, frs, lane == 0 ? bid * 4 : OOB, 0, 16);
    tagv = tag; oldv = e0;
  } else {
    if (wave == 1 && lane == 0) *trig = 0u;
    lds_barrier();
  }
  if (dutyF) bv = bf2f(b1[nj * 64 + lane]);
  else if (dutyR) { xv = bf2f(x[(nj - NF) * 32 + (lane & 31)]); bv = bf2f(b2[(nj - NF) * 32 + (lane & 31)]); }
  asm volatile("" : "+v"(xv), "+v"(bv));
  const int nblk = wave == 0 ? 3 : 2;
  if (wave == 0) {
    while (*trig == 0u) __builtin_amdgcn_s_sleep(32);
    auto frs = RSRC(ws + WS_FLAGS, 256 * 4);
    for (int spin = 0;; ++spin) {
      asm volatile("" ::: "memory");
      const u32x4 fl = __builtin_amdgcn_raw_buffer_load_b128(frs, lane * 16, 0, 16);
      const unsigned bad = (fl[0] ^ tagv) | (fl[1] ^ tagv) | (fl[2] ^ tagv) | (fl[3] ^ tagv);
      if (__all(bad == 0) || spin > SPIN_MAX) break;
      __builtin_amdgcn_s_sleep(64);
    }
    auto ers = RSRC(ws + WS_EPOCH, 4);
    __builtin_amdgcn_raw_buffer_store_b32(tagv, ers, (bid == 0 && lane == 0) ? 0 : OOB, 0, 16);
    auto qrs = RSRC(ws + WS_QX + (size_t)g * (4 * 256 * 8), 4 * 256 * 8);
    __builtin_amdgcn_raw_buffer_store_b128(u32x4{0u, oldv, 0u, oldv}, qrs, vlane ? ((sp >> 2) * 256 + (sp & 3) * 64 + vcol) * 8 : OOB, 0, 16);
  }
  u32x4 WA[18], WB[24];
  if (dutyA) {
    while (*trig == 0u) __builtin_amdgcn_s_sleep(64);
    while (*trig != 2u) __builtin_amdgcn_s_sleep(8);
    auto wrs = RSRC(wo + (size_t)(g * 256 + wave * 64) * D + sp * 64, 64 * D * 2);
#pragma unroll
    for (int i = 0; i < 8; ++i) WA[i] = __builtin_amdgcn_raw_buffer_load_b128(wrs, rl * (D * 2) + ch * 16, i * (8 * D * 2), 2);
  }
  lds_barrier();
  {
    const int h = tid >> 7;
    float mw[NSW], M = -INFINITY;
#pragma unroll
    for (int w = 0; w < NSW; ++w) { mw[w] = mrg[w * PART_STRIDE + 256 + h]; M = fmaxf(M, mw[w]); }
    float L = 0.f, o = 0.f;
#pragma unroll
    for (int w = 0; w < NSW; ++w) {
      const float wt = __builtin_amdgcn_exp2f((mw[w] - M) * LOG2E);
      L = fmaf(wt, mrg[w * PART_STRIDE + 258 + h], L); o = fmaf(wt, mrg[w * PART_STRIDE + tid], o);
    }
    __builtin_amdgcn_raw_buffer_store_b32(canon(o), part_rs, (bid * PART_STRIDE + tid) * 4, 0, 16);
    __builtin_amdgcn_raw_buffer_store_b32(canon((lane & 1) ? L : M), part_rs, ((tid & 126) == 0) ? (bid * PART_STRIDE + 256 + h + 2 * (lane & 1)) * 4 : OOB, 0, 16);
  }
  if (dutyF) {
    auto w1rs = RSRC(w1 + (size_t)(wave * 144) * F + nj * 64, 144 * F * 2);
#pragma unroll
    for (int i = 0; i < 18; ++i) WA[i] = __builtin_amdgcn_raw_buffer_load_b128(w1rs, rl * (F * 2) + ch * 16, i * (8 * F * 2), 2);
    auto w2rs = RSRC(w2 + (size_t)(nj * 64) * D, 64 * D * 2);
#pragma unroll
    for (int b = 0; b < 3; ++b)
#pragma unroll
      for (int i = 0; i < 8; ++i)
        WB[b * 8 + i] = __builtin_amdgcn_raw_buffer_load_b128(w2rs, b < nblk ? rl * (D * 2) + ch * 16 + (wave + 4 * b) * 128 : OOB, i * (8 * D * 2), 2);
  }
  if (dutyA) {
    const int mo = ((g * NSPLIT + (lane & 15)) * PART_STRIDE + 256 + (wave >> 1)) * 4;
    unsigned mu = SENT, lu = SENT, ov[16];
#pragma unroll
    for (int s = 0; s < 16; ++s) ov[s] = SENT;
    for (int spin = 0; NEAR_A < 16; ++spin) {
      asm volatile("" ::: "memory");
      mu = __builtin_amdgcn_raw_buffer_load_b32(part_rs, lane < 16 ? mo : OOB, 0, 16);
      if (__builtin_popcountll(__ballot(mu == SENT)) <= NEAR_A || spin > SPIN_MAX) break;
      __builtin_amdgcn_s_sleep(SLEEP_FAR);
    }
    mu = SENT;
    for (int spin = 0;; ++spin) {
      asm volatile("" ::: "memory");
      if (max(mu, lu) == SENT) { mu = __builtin_amdgcn_raw_buffer_load_b32(part_rs, mo, 0, 16); lu = __builtin_amdgcn_raw_buffer_load_b32(part_rs, mo + 8, 0, 16); }
#pragma unroll
      for (int s = 0; s < 16; ++s)
        if (ov[s] == SENT) ov[s] = __builtin_amdgcn_raw_buffer_load_b32(part_rs, (g * NSPLIT * PART_STRIDE + tid) * 4, s * PART_STRIDE * 4, 16);
      unsigned mx = max(mu, lu);
#pragma unroll
      for (int s = 0; s < 16; s += 2) mx = umax3(mx, ov[s], ov[s + 1]);
      if (__all(mx != SENT) || spin > SPIN_MAX) break;
      if (SLEEP_NEAR) __builtin_amdgcn_s_sleep(SLEEP_NEAR);
    }
    const float mv = u2f(mu);
    const float M = row_max(mv);
    const float wt = __builtin_amdgcn_exp2f((mv - M) * LOG2E);
    const float L = row_sum(wt * u2f(lu));
    float o = 0.f, o_ = 0.f;
#define CMB(s) o = fmaf(dpp<0x150 + (s)>(wt), u2f(ov[s]), o); o_ = fmaf(dpp<0x150 + (s) + 1>(wt), u2f(ov[(s) + 1]), o_);
    CMB(0) CMB(2) CMB(4) CMB(6) CMB(8) CMB(10) CMB(12) CMB(14)
#undef CMB
    o = fdiv(o + o_, L, __builtin_amdgcn_rcpf(L));
    const unsigned o2 = pk2(o, o);
    unsigned act[8];
#pragma unroll
    for (int i = 0; i < 8; ++i) act[i] = bperm(8 * i + rl, o2);
    __builtin_amdgcn_sched_barrier(0);
    float r0, r1; gemv_fold(gemv_chain<8>(WA, act, msk), r0, r1);
    if (vlane) *reinterpret_cast<float2*>(red + wave * 64 + vcol) = make_float2(r0, r1);
    lds_barrier();
    if (tid < 16) {
      f32x4 a = *reinterpret_cast<const f32x4*>(red + tid * 4);
#pragma unroll
      for (int w = 1; w < 4; ++w) a += *reinterpret_cast<const f32x4*>(red + w * 64 + tid * 4);
      __builtin_amdgcn_raw_buffer_store_b128(u32x4{canon(a[0]), canon(a[1]), canon(a[2]), canon(a[3])}, p2rs, (g * D + sp * 64 + tid * 4) * 4, 0, 16);
    }
  } else if (dutyF) {
    lds_barrier();
    float hv[9], hr[3];
#pragma unroll
    for (int k = 0; k < 9; ++k) hv[k] = hs[lane + 64 * k];
#pragma unroll
    for (int k = 0; k < 3; ++k) hr[k] = hs[min(wave * 144 + lane + 64 * k, D - 1)];
    const float c0 = u2f(__builtin_amdgcn_readfirstlane(f2u(hv[0])));
    float s1 = 0.f, s2 = 0.f;
#pragma unroll
    for (int k = 0; k < 9; ++k) { const float d = hv[k] - c0; s1 += d; s2 = fmaf(d, d, s2); }
    s1 = wave_sum(s1) * (1.0f / D); s2 = wave_sum(s2) * (1.0f / D);
    const float var = rbf(fmaxf(fmaf(-s1, s1, s2), 0.f));
    const float mb = rbf(c0 + s1), den = rbf(__builtin_amdgcn_sqrtf(rbf(var + 1e-5f))), rden = __builtin_amdgcn_rcpf(den);
    unsigned hn2[3];
#pragma unroll
    for (int k = 0; k < 3; ++k) { const float t = fdiv(rbf(hr[k] - mb), den, rden); hn2[k] = pk2(t, t); }
    {
      unsigned act[18];
#pragma unroll
      for (int i = 0; i < 18; ++i) act[i] = bperm((8 * i + rl) & 63, hn2[(8 * i) >> 6]);
      __builtin_amdgcn_sched_barrier(0);
      float r0, r1; gemv_fold(gemv_chain<18>(WA, act, msk), r0, r1);
      if (vlane) *reinterpret_cast<float2*>(red + wave * 64 + vcol) = make_float2(r0, r1);
    }
    lds_barrier();
    {
      float hid = rbf(rbf((red[lane] + red[64 + lane]) + (red[128 + lane] + red[192 + lane])) + bv);
      hid = hid > 0.f ? hid : 0.f;
      hid = hid * hid;
      const unsigned hid2 = pk2(hid, hid);
      unsigned act[8];
#pragma unroll
      for (int i = 0; i < 8; ++i) act[i] = bperm(8 * i + rl, hid2);
      __builtin_amdgcn_sched_barrier(0);
      f32x4 acc[3] = {{0.f, 0.f, 0.f, 0.f}, {0.f, 0.f, 0.f, 0.f}, {0.f, 0.f, 0.f, 0.f}};
      u32x4 a[8];
      a[0] = onehot(act[0], msk);
#pragma unroll
      for (int i = 0; i < 8; ++i) {
        if (i + 1 < 8) { a[i + 1] = onehot(act[i + 1], msk); asm volatile("" : "+v"(a[i + 1])); }
#pragma unroll
        for (int b = 0; b < 3; ++b) acc[b] = MFMA(a[i], WB[b * 8 + i], acc[b]);
        __builtin_amdgcn_sched_barrier(0);
      }
#pragma unroll
      for (int b = 0; b < 3; ++b) {
        float r0, r1; gemv_fold(acc[b], r0, r1);
        __builtin_amdgcn_raw_buffer_store_b64(u32x2{canon(r0), canon(r1)}, p3rs, (vlane && b < nblk) ? (nj * D + (wave + 4 * b) * 64 + vcol) * 4 : OOB, 0, 16);
      }
    }
  } else if (dutyR) {
    const int rc0 = (nj - NF) * 32;
    float hv = 0.f;
    {
      unsigned pv[16];
#pragma unroll
      for (int k = 0; k < 16; ++k) pv[k] = SENT;
      for (int spin = 0;; ++spin) {
        asm volatile("" ::: "memory");
#pragma unroll
        for (int k = 0; k < 16; ++k)
          if (pv[k] == SENT) pv[k] = __builtin_amdgcn_raw_buffer_load_b32(p2rs, (rc0 + (lane & 31)) * 4, k * D * 4, 16);
        unsigned mx = max(pv[0], pv[1]);
#pragma unroll
        for (int k = 2; k < 16; k += 2) mx = umax3(mx, pv[k], pv[k + 1]);
        if (__all(mx != SENT) || spin > SPIN_MAX) break;
        __builtin_amdgcn_s_sleep(SLEEP_FAR);
      }
      float t[8];
#pragma unroll
      for (int k = 0; k < 8; ++k) t[k] = u2f(pv[k]) + u2f(pv[k + 8]);
      hv = rbf(xv + rbf(((t[0] + t[1]) + (t[2] + t[3])) + ((t[4] + t[5]) + (t[6] + t[7]))));
    }
    const int off = ((tid >> 3) * D + rc0 + ch * 4) * 4;
    f32x4 v;
    for (int spin = 0;; ++spin) {
      asm volatile("" ::: "memory");
      const u32x4 u0 = __builtin_amdgcn_raw_buffer_load_b128(p3rs, off, 0, 16);
      const u32x4 u1 = __builtin_amdgcn_raw_buffer_load_b128(p3rs, off, 32 * D * 4, 16);
      const u32x4 u2 = __builtin_amdgcn_raw_buffer_load_b128(p3rs, tid < 64 ? off + 64 * D * 4 : OOB, 0, 16);
      unsigned mx = max(umax3(u0[0], u0[1], u0[2]), u0[3]);
      mx = max(umax3(mx, u1[0], u1[1]), max(u1[2], u1[3]));
      mx = max(umax3(mx, u2[0], u2[1]), max(u2[2], u2[3]));
      v = (__builtin_bit_cast(f32x4, u0) + __builtin_bit_cast(f32x4, u1)) + __builtin_bit_cast(f32x4, u2);
      if (__all(mx != SENT) || spin > SPIN_MAX) break;
    }
#pragma unroll
    for (int k = 0; k < 4; ++k) { float a = v[k]; a += dpp<XOR8>(a); v[k] = xrow_sum(a); }
    if (lane < 8) *reinterpret_cast<f32x4*>(red + wave * 32 + lane * 4) = v;
    lds_barrier();
    if (wave == 3 && lane < 32) {
      const float a = (red[lane] + red[32 + lane]) + (red[64 + lane] + red[96 + lane]);
      auto ors = RSRC(out, D * 2);
      __builtin_amdgcn_raw_buffer_store_b16(f2bf(hv + rbf(rbf(a) + bv)), ors, (rc0 + lane) * 2, 0, 0);
    }
    if (wave == 0 && nj == NF) {
      auto frs = RSRC(ws + WS_FLAGS, 256 * 4);
      __builtin_amdgcn_raw_buffer_store_b128(u32x4{oldv, oldv, oldv, oldv}, frs, lane * 16, 0, 16);
    }
  }
}

extern "C" void kernel_launch(void* const* d_in, const int* in_sizes, int n_in, void* d_out, int out_size, void* d_ws, size_t ws_size, hipStream_t stream) {
  k_fused<<<HKV * NSPLIT, 512, 0, stream>>>((const u16*)d_in[0], (const u16*)d_in[1], (const u16*)d_in[2], (const u16*)d_in[3], (const u16*)d_in[4],
                                            (const u16*)d_in[6], (const u16*)d_in[7], (const u16*)d_in[8], (const u16*)d_in[9], (unsigned char*)d_ws, (u16*)d_out);
}
```
